```python
import torch

def _softmax(x, axis=-1):
    return torch.softmax(x, dim=axis)
def _gelu(x):
    return torch.nn.functional.gelu(x, approximate="tanh")
def _silu(x):
    return torch.nn.functional.silu(x)
def _sigmoid(x):
    return torch.sigmoid(x)
def _layernorm(x, axis=-1, eps=1e-5):
    m = x.mean(dim=axis, keepdim=True)
    v = x.var(dim=axis, keepdim=True, unbiased=False)
    return (x - m) / torch.sqrt(v + eps)
def _scatter_add(tgt, idx, src, axis=0):
    return tgt.clone().index_add_(axis, idx, src)
def _segment_sum(src, seg, n):
    out = torch.zeros((n,) + src.shape[1:], dtype=src.dtype)
    return out.index_add_(0, seg, src)
def _pad(x, pw):
    flat = tuple(v for pair in reversed(pw) for v in pair)
    return torch.nn.functional.pad(x, flat)

kernel_name = 'parallel_sum__sliding_block__moe_arch_mod__prompt'

def setup_inputs():

    _w = (lambda a: (lambda t: t.to(torch.bfloat16) if t.is_floating_point() else t)(torch.as_tensor(a)))
    def _A_setup():
        B, S, D = 1, 2048, 448
        return {
            "x": _w((lambda *s: torch.randn(*s, dtype=torch.bfloat16) * 0.05)(B, S, D)),
            "wqkv": _w((lambda *s: torch.randn(*s, dtype=torch.bfloat16) * 0.05)(D, 3 * D)),
            "wo": _w((lambda *s: torch.randn(*s, dtype=torch.bfloat16) * 0.05)(D, D)),
        }
    _A_in = _A_setup()

    def _B_setup():
        B, S, D, F, C = 1, 2048, 448, 1792, 2
        return {
            "x": _w((lambda *s: torch.randn(*s, dtype=torch.bfloat16) * 0.05)(B, S, D)),
            "w_r": _w((lambda *s: torch.randn(*s, dtype=torch.bfloat16) * 0.05)(D)),
            "w_gate": _w((lambda *s: torch.randn(*s, dtype=torch.bfloat16) * 0.05)(D, F)),
            "w_up": _w((lambda *s: torch.randn(*s, dtype=torch.bfloat16) * 0.05)(D, F)),
            "w_down": _w((lambda *s: torch.randn(*s, dtype=torch.bfloat16) * 0.05)(F, D)),
        }
    _B_in = _B_setup()

    return {
        "A_x": _A_in["x"],
        "A_wqkv": _A_in["wqkv"],
        "A_wo": _A_in["wo"],
        "B_x": _B_in["x"],
        "B_w_r": _B_in["w_r"],
        "B_w_gate": _B_in["w_gate"],
        "B_w_up": _B_in["w_up"],
        "B_w_down": _B_in["w_down"],
    }

def reference(A_x, A_wqkv, A_wo, B_x, B_w_r, B_w_gate, B_w_up, B_w_down):
    def _A_ref(x, wqkv, wo):
        B, S, D = x.shape
        H = 16; Dh = D // H; W = 256
        qkv = (lambda x, *d: x.permute(*d))((x @ wqkv).reshape(B, S, 3, H, Dh), 2, 0, 3, 1, 4)
        q, k, v = qkv[0], qkv[1], qkv[2]
        scores = torch.einsum("bhqd,bhkd->bhqk", q, k) * (Dh ** -0.5)
        idx = torch.arange(S)
        mask = (idx[:, None] - idx[None, :] >= 0) & (idx[:, None] - idx[None, :] < W)
        scores = torch.where(mask[None, None], scores, -1e9)
        p = _softmax(scores, axis=-1)
        o = torch.einsum("bhqk,bhkd->bhqd", p, v)
        return (lambda x, *d: x.permute(*d))(o, 0, 2, 1, 3).reshape(B, S, D) @ wo

    def _B_ref(x, w_r, w_gate, w_up, w_down):
        B, S, D = x.shape
        C = 2
        r = x @ w_r
        idx = torch.argsort(-r, axis=-1)[:, :C]
        idx_e = (lambda x, n, axis: torch.repeat_interleave(x, n, dim=axis))(idx.reshape(B, C, 1), D, axis=2)
        x_sel = (lambda x, i, axis: torch.take_along_dim(x, i, dim=axis))(x, idx_e, axis=1)
        g = _silu(x_sel @ w_gate)
        u = x_sel @ w_up
        y = (g * u) @ w_down
        r_sel = _sigmoid((lambda x, i, axis: torch.take_along_dim(x, i, dim=axis))(r, idx, axis=1))
        y = y * r_sel.reshape(B, C, 1)
        b_off = (torch.arange(B) * S).reshape(B, 1)
        flat_idx = (idx + b_off).reshape(B * C)
        zero = (x * 0.0).reshape(B * S, D)
        upd = _scatter_add(zero, flat_idx, y.reshape(B * C, D), axis=0)
        return x + upd.reshape(B, S, D)

    x = A_x
    xn = _layernorm(x, axis=-1)
    return x + _A_ref(xn, A_wqkv, A_wo) + _B_ref(xn, B_w_r, B_w_gate, B_w_up, B_w_down)
```

```cpp
#include <hip/hip_runtime.h>
#include <hip/hip_bf16.h>
#include <stdint.h>

constexpr int S_ = 2048, D_ = 448, H_ = 16, DH = 28, WIN = 256, F_ = 1792;
constexpr int N3 = 3 * D_;

typedef short bf16x8 __attribute__((ext_vector_type(8)));
typedef short s16x4 __attribute__((ext_vector_type(4)));
typedef float f32x4 __attribute__((ext_vector_type(4)));
typedef unsigned u32x4 __attribute__((ext_vector_type(4)));
typedef unsigned u32x2 __attribute__((ext_vector_type(2)));

__device__ __forceinline__ float bf2f(unsigned short u) { return __uint_as_float(((unsigned)u) << 16); }
__device__ __forceinline__ unsigned short f2bf(float f) {
  __hip_bfloat16 h = __float2bfloat16(f);
  return *reinterpret_cast<unsigned short*>(&h);
}
__device__ __forceinline__ unsigned pack2(float lo, float hi) {
  return (unsigned)f2bf(lo) | ((unsigned)f2bf(hi) << 16);
}
__device__ __forceinline__ float wave_sum(float v) {
#pragma unroll
  for (int o = 32; o >= 1; o >>= 1) v += __shfl_xor(v, o, 64);
  return v;
}

constexpr size_t WS_QB = 0;
constexpr size_t WS_KB = WS_QB + (size_t)H_ * S_ * 32 * 2;
constexpr size_t WS_VT = WS_KB + (size_t)H_ * S_ * 32 * 2;
constexpr size_t WS_O  = WS_VT + (size_t)H_ * 32 * S_ * 2;
constexpr size_t WS_ST = WS_O + (size_t)S_ * D_ * 2;
constexpr size_t WS_R  = WS_ST + (size_t)S_ * 8;
constexpr int MOE_NS = 56;
constexpr size_t WS_MP = WS_R + (size_t)S_ * 4;
constexpr size_t WS_SEL = WS_MP + (size_t)MOE_NS * 2 * D_ * 4;

constexpr int K1_BM = 64, K1_BN = 192, K1_KC = 64;
constexpr int K1_AS = D_ + 8;
constexpr int K1_WS = K1_BN + 8;

__global__ __launch_bounds__(256) void k1_ln_qkv(const unsigned short* __restrict__ x,
                                                  const unsigned short* __restrict__ wqkv,
                                                  const unsigned short* __restrict__ w_r,
                                                  unsigned char* __restrict__ ws) {
  __shared__ __attribute__((aligned(16))) unsigned short sA[K1_BM * K1_AS];
  __shared__ __attribute__((aligned(16))) unsigned short sW[2][K1_KC * K1_WS];
  const int tid = threadIdx.x, lane = tid & 63, wave = tid >> 6;
  const int row0 = blockIdx.x * K1_BM;
  const int col0 = blockIdx.y * K1_BN;

  u32x4 wreg[6];
#pragma unroll
  for (int i = 0; i < 6; ++i) {
    int c = tid + i * 256;
    int r = c / 24, cc = c % 24;
    wreg[i] = *reinterpret_cast<const u32x4*>(wqkv + (size_t)r * N3 + col0 + cc * 8);
  }

  {
    u32x4 xr[16];
#pragma unroll
    for (int i = 0; i < 16; ++i) {
      int r = row0 + wave * 16 + i;
      if (lane < 56) xr[i] = *reinterpret_cast<const u32x4*>(x + (size_t)r * D_ + lane * 8);
      else xr[i] = u32x4{0u, 0u, 0u, 0u};
    }
    float wr[8];
    if (blockIdx.y == 0) {
      u32x4 w4 = (lane < 56) ? *reinterpret_cast<const u32x4*>(w_r + lane * 8) : u32x4{0u, 0u, 0u, 0u};
#pragma unroll
      for (int j = 0; j < 4; ++j) { wr[2 * j] = bf2f(w4[j] & 0xffff); wr[2 * j + 1] = bf2f(w4[j] >> 16); }
    }
#pragma unroll
    for (int i = 0; i < 16; ++i) {
      float v[8];
#pragma unroll
      for (int j = 0; j < 4; ++j) { v[2 * j] = bf2f(xr[i][j] & 0xffff); v[2 * j + 1] = bf2f(xr[i][j] >> 16); }
      float s = 0.f;
#pragma unroll
      for (int j = 0; j < 8; ++j) s += v[j];
      s = wave_sum(s);
      float mean = s * (1.f / D_);
      float q = 0.f;
#pragma unroll
      for (int j = 0; j < 8; ++j) { float d = (lane < 56) ? (v[j] - mean) : 0.f; v[j] = d; q += d * d; }
      q = wave_sum(q);
      float rstd = rsqrtf(q * (1.f / D_) + 1e-5f);
      u32x4 o;
#pragma unroll
      for (int j = 0; j < 4; ++j) o[j] = pack2(v[2 * j] * rstd, v[2 * j + 1] * rstd);
      int rl = wave * 16 + i;
      if (lane < 56) *reinterpret_cast<u32x4*>(&sA[rl * K1_AS + lane * 8]) = o;
      if (blockIdx.y == 0) {
        float rr = 0.f;
#pragma unroll
        for (int j = 0; j < 4; ++j) { rr += bf2f(o[j] & 0xffff) * wr[2 * j] + bf2f(o[j] >> 16) * wr[2 * j + 1]; }
        rr = wave_sum(rr);
        if (lane == 0) {
          reinterpret_cast<float*>(ws + WS_R)[row0 + rl] = rr;
          reinterpret_cast<float2*>(ws + WS_ST)[row0 + rl] = make_float2(mean, rstd);
        }
      }
    }
  }
#pragma unroll
  for (int i = 0; i < 6; ++i) {
    int c = tid + i * 256; int r = c / 24, cc = c % 24;
    *reinterpret_cast<u32x4*>(&sW[0][r * K1_WS + cc * 8]) = wreg[i];
  }
  __syncthreads();

  f32x4 acc[4][3];
#pragma unroll
  for (int m = 0; m < 4; ++m)
#pragma unroll
    for (int n = 0; n < 3; ++n) acc[m][n] = f32x4{0.f, 0.f, 0.f, 0.f};

  const int g = lane >> 4, l15 = lane & 15;
  const int wcol = wave * 48;
  constexpr int NCH = D_ / K1_KC;
  for (int ch = 0; ch < NCH; ++ch) {
    const int buf = ch & 1;
    if (ch + 1 < NCH) {
#pragma unroll
      for (int i = 0; i < 6; ++i) {
        int c = tid + i * 256; int r = c / 24, cc = c % 24;
        wreg[i] = *reinterpret_cast<const u32x4*>(wqkv + (size_t)((ch + 1) * K1_KC + r) * N3 + col0 + cc * 8);
      }
    }
#pragma unroll
    for (int ks = 0; ks < 2; ++ks) {
      bf16x8 a[4], b[3];
#pragma unroll
      for (int m = 0; m < 4; ++m)
        a[m] = *reinterpret_cast<const bf16x8*>(&sA[(m * 16 + l15) * K1_AS + ch * K1_KC + ks * 32 + g * 8]);
#pragma unroll
      for (int n = 0; n < 3; ++n) {
        const int q = l15 >> 2, p = l15 & 3;
        const unsigned short* base = &sW[buf][(ks * 32 + 8 * g + q) * K1_WS + wcol + n * 16 + p * 4];
        s16x4 t0 = __builtin_amdgcn_ds_read_tr16_b64_v4i16((__attribute__((address_space(3))) s16x4*)(base));
        s16x4 t1 = __builtin_amdgcn_ds_read_tr16_b64_v4i16((__attribute__((address_space(3))) s16x4*)(base + 4 * K1_WS));
        b[n] = bf16x8{t0[0], t0[1], t0[2], t0[3], t1[0], t1[1], t1[2], t1[3]};
      }
#pragma unroll
      for (int n = 0; n < 3; ++n) {
        if (col0 + wcol + n * 16 < 2 * D_) {
#pragma unroll
          for (int m = 0; m < 4; ++m) acc[m][n] = __builtin_amdgcn_mfma_f32_16x16x32_bf16(b[n], a[m], acc[m][n], 0, 0, 0);
        } else {
#pragma unroll
          for (int m = 0; m < 4; ++m) acc[m][n] = __builtin_amdgcn_mfma_f32_16x16x32_bf16(a[m], b[n], acc[m][n], 0, 0, 0);
        }
      }
    }
    if (ch + 1 < NCH) {
#pragma unroll
      for (int i = 0; i < 6; ++i) {
        int c = tid + i * 256; int r = c / 24, cc = c % 24;
        *reinterpret_cast<u32x4*>(&sW[buf ^ 1][r * K1_WS + cc * 8]) = wreg[i];
      }
    }
    __syncthreads();
  }

  unsigned short* qb = reinterpret_cast<unsigned short*>(ws + WS_QB);
  unsigned short* vt = reinterpret_cast<unsigned short*>(ws + WS_VT);
#pragma unroll
  for (int n = 0; n < 3; ++n) {
    const int ncol = col0 + wcol + n * 16;
    if (ncol < 2 * D_) {
      const int nn = ncol + 4 * g;
      const int part = nn / D_;
      const int rem = nn - part * D_;
      const int h = rem / DH, d = rem - h * DH;
      unsigned short* dst = qb + (size_t)part * (H_ * S_ * 32) + (size_t)h * S_ * 32;
#pragma unroll
      for (int m = 0; m < 4; ++m) {
        const int s = row0 + m * 16 + l15;
        u32x2 v; v[0] = pack2(acc[m][n][0], acc[m][n][1]); v[1] = pack2(acc[m][n][2], acc[m][n][3]);
        *reinterpret_cast<u32x2*>(dst + (size_t)s * 32 + d) = v;
        if (d == 24) *reinterpret_cast<u32x2*>(dst + (size_t)s * 32 + 28) = u32x2{0u, 0u};
      }
    } else {
      const int rem = ncol + l15 - 2 * D_;
      const int h = rem / DH, d = rem - h * DH;
      unsigned short* dst = vt + ((size_t)h * 32 + d) * S_;
#pragma unroll
      for (int m = 0; m < 4; ++m) {
        const int s = row0 + m * 16 + 4 * g;
        u32x2 v; v[0] = pack2(acc[m][n][0], acc[m][n][1]); v[1] = pack2(acc[m][n][2], acc[m][n][3]);
        *reinterpret_cast<u32x2*>(dst + s) = v;
      }
    }
  }
}

constexpr int K2_ATT_BLOCKS = (S_ / 64) * H_;

__device__ void moe_block(int slice, const unsigned short* __restrict__ x,
                          const unsigned short* __restrict__ w_gate, const unsigned short* __restrict__ w_up,
                          const unsigned short* __restrict__ w_down, unsigned char* __restrict__ ws) {
  __shared__ float s_red[4][4];
  __shared__ int s_redi[4][2];
  __shared__ float s_xn[2][D_];
  __shared__ float s_part[16][4][32];
  __shared__ float s_h[2][32];
  const int tid = threadIdx.x, lane = tid & 63, wave = tid >> 6;
  const int f0 = slice * 32;
  const float* r = reinterpret_cast<const float*>(ws + WS_R);
  float b1 = -INFINITY, b2 = -INFINITY; int i1 = 0x7fffffff, i2 = 0x7fffffff;
#pragma unroll
  for (int i = 0; i < 8; ++i) {
    int idx = tid * 8 + i;
    float v = r[idx];
    if (v > b1) { b2 = b1; i2 = i1; b1 = v; i1 = idx; }
    else if (v > b2) { b2 = v; i2 = idx; }
  }
#pragma unroll
  for (int o = 1; o < 64; o <<= 1) {
    float c1 = __shfl_xor(b1, o, 64), c2 = __shfl_xor(b2, o, 64);
    int j1 = __shfl_xor(i1, o, 64), j2 = __shfl_xor(i2, o, 64);
    bool cfirst = (c1 > b1) || (c1 == b1 && j1 < i1);
    float n1 = cfirst ? c1 : b1; int m1 = cfirst ? j1 : i1;
    float xa = cfirst ? b1 : c1; int ia = cfirst ? i1 : j1;
    float xb = cfirst ? c2 : b2; int ib = cfirst ? j2 : i2;
    bool asec = (xa > xb) || (xa == xb && ia < ib);
    b1 = n1; i1 = m1; b2 = asec ? xa : xb; i2 = asec ? ia : ib;
  }
  if (lane == 0) { s_red[wave][0] = b1; s_red[wave][1] = b2; s_redi[wave][0] = i1; s_redi[wave][1] = i2; }
  __syncthreads();
  b1 = s_red[0][0]; b2 = s_red[0][1]; i1 = s_redi[0][0]; i2 = s_redi[0][1];
#pragma unroll
  for (int w = 1; w < 4; ++w) {
    float c1 = s_red[w][0], c2 = s_red[w][1]; int j1 = s_redi[w][0], j2 = s_redi[w][1];
    bool cfirst = (c1 > b1) || (c1 == b1 && j1 < i1);
    float n1 = cfirst ? c1 : b1; int m1 = cfirst ? j1 : i1;
    float xa = cfirst ? b1 : c1; int ia = cfirst ? i1 : j1;
    float xb = cfirst ? c2 : b2; int ib = cfirst ? j2 : i2;
    bool asec = (xa > xb) || (xa == xb && ia < ib);
    b1 = n1; i1 = m1; b2 = asec ? xa : xb; i2 = asec ? ia : ib;
  }
  const int sel[2] = {i1, i2};
  const float rs[2] = {b1, b2};
  const float2* st = reinterpret_cast<const float2*>(ws + WS_ST);
  for (int e = tid; e < 2 * D_; e += 256) {
    int t = e / D_, c = e - t * D_;
    float2 ms = st[sel[t]];
    float v = (bf2f(x[(size_t)sel[t] * D_ + c]) - ms.x) * ms.y;
    s_xn[t][c] = bf2f(f2bf(v));
  }
  __syncthreads();
  {
    const int kg = tid >> 4, cp = tid & 15;
    float a[4][2] = {{0, 0}, {0, 0}, {0, 0}, {0, 0}};
#pragma unroll 4
    for (int kk = 0; kk < 28; ++kk) {
      int k = kg * 28 + kk;
      unsigned wg = *reinterpret_cast<const unsigned*>(w_gate + (size_t)k * F_ + f0 + cp * 2);
      unsigned wu = *reinterpret_cast<const unsigned*>(w_up + (size_t)k * F_ + f0 + cp * 2);
      float g0 = bf2f(wg & 0xffff), g1 = bf2f(wg >> 16), u0 = bf2f(wu & 0xffff), u1 = bf2f(wu >> 16);
      float x0 = s_xn[0][k], x1 = s_xn[1][k];
      a[0][0] += x0 * g0; a[0][1] += x0 * g1; a[1][0] += x0 * u0; a[1][1] += x0 * u1;
      a[2][0] += x1 * g0; a[2][1] += x1 * g1; a[3][0] += x1 * u0; a[3][1] += x1 * u1;
    }
#pragma unroll
    for (int j = 0; j < 4; ++j) { s_part[kg][j][cp * 2] = a[j][0]; s_part[kg][j][cp * 2 + 1] = a[j][1]; }
  }
  __syncthreads();
  if (tid < 64) {
    int t = tid >> 5, c = tid & 31;
    float gs = 0.f, us = 0.f;
#pragma unroll
    for (int kg = 0; kg < 16; ++kg) { gs += s_part[kg][t * 2][c]; us += s_part[kg][t * 2 + 1][c]; }
    float sg = gs / (1.f + __expf(-gs));
    s_h[t][c] = sg * us;
  }
  __syncthreads();
  if (tid < D_ / 2) {
    float y[2][2] = {{0, 0}, {0, 0}};
#pragma unroll 8
    for (int f = 0; f < 32; ++f) {
      unsigned wd = *reinterpret_cast<const unsigned*>(w_down + (size_t)(f0 + f) * D_ + tid * 2);
      float d0 = bf2f(wd & 0xffff), d1 = bf2f(wd >> 16);
      y[0][0] += s_h[0][f] * d0; y[0][1] += s_h[0][f] * d1;
      y[1][0] += s_h[1][f] * d0; y[1][1] += s_h[1][f] * d1;
    }
    float* mp = reinterpret_cast<float*>(ws + WS_MP) + (size_t)slice * 2 * D_;
#pragma unroll
    for (int t = 0; t < 2; ++t) {
      float sc = 1.f / (1.f + __expf(-rs[t]));
      *reinterpret_cast<float2*>(mp + t * D_ + tid * 2) = make_float2(y[t][0] * sc, y[t][1] * sc);
    }
  }
  if (slice == 0 && tid == 0) {
    int* selp = reinterpret_cast<int*>(ws + WS_SEL);
    selp[0] = sel[0]; selp[1] = sel[1];
  }
}

__global__ __launch_bounds__(256) void k2_attn(const unsigned short* __restrict__ x,
                                                const unsigned short* __restrict__ w_gate,
                                                const unsigned short* __restrict__ w_up,
                                                const unsigned short* __restrict__ w_down,
                                                unsigned char* __restrict__ ws) {
  if (blockIdx.x >= K2_ATT_BLOCKS) {
    moe_block(blockIdx.x - K2_ATT_BLOCKS, x, w_gate, w_up, w_down, ws);
    return;
  }
  const int tid = threadIdx.x, lane = tid & 63, wave = tid >> 6;
  const int g = lane >> 4, l15 = lane & 15;
  const int h = blockIdx.x & 15;
  const int qt = blockIdx.x >> 4;
  const int q0 = qt * 64 + wave * 16;
  const unsigned short* qb = reinterpret_cast<const unsigned short*>(ws + WS_QB) + (size_t)h * S_ * 32;
  const unsigned short* kb = reinterpret_cast<const unsigned short*>(ws + WS_KB) + (size_t)h * S_ * 32;
  const unsigned short* vt = reinterpret_cast<const unsigned short*>(ws + WS_VT) + (size_t)h * 32 * S_;

  const bf16x8 qf = *reinterpret_cast<const bf16x8*>(qb + (size_t)(q0 + l15) * 32 + g * 8);
  const int kbase = q0 - WIN;
  bf16x8 kf[17];
#pragma unroll
  for (int j = 0; j < 17; ++j) {
    int key = kbase + 16 * j + l15;
    key = key < 0 ? 0 : key;
    kf[j] = *reinterpret_cast<const bf16x8*>(kb + (size_t)key * 32 + g * 8);
  }
  f32x4 sc[18];
#pragma unroll
  for (int j = 0; j < 17; ++j)
    sc[j] = __builtin_amdgcn_mfma_f32_16x16x32_bf16(kf[j], qf, f32x4{0.f, 0.f, 0.f, 0.f}, 0, 0, 0);
  float mx = -INFINITY;
#pragma unroll
  for (int j = 0; j < 17; ++j) {
    const bool tile_ok = (kbase + 16 * j) >= 0;
#pragma unroll
    for (int i = 0; i < 4; ++i) {
      const int b = 4 * g + i;
      bool ok = tile_ok;
      if (j == 0) ok = ok && (b >= l15 + 1);
      if (j == 16) ok = ok && (b <= l15);
      sc[j][i] = ok ? sc[j][i] : -INFINITY;
      mx = fmaxf(mx, sc[j][i]);
    }
  }
  sc[17] = f32x4{-INFINITY, -INFINITY, -INFINITY, -INFINITY};
  mx = fmaxf(mx, __shfl_xor(mx, 16, 64));
  mx = fmaxf(mx, __shfl_xor(mx, 32, 64));
  const float cs = 0.18898223650461362f * 1.4426950408889634f;
  const float mxs = mx * cs;
  float sum = 0.f;
  bf16x8 pf[9];
#pragma unroll
  for (int p = 0; p < 9; ++p) {
    float e[8];
#pragma unroll
    for (int i = 0; i < 4; ++i) {
      e[i] = exp2f(sc[2 * p][i] * cs - mxs);
      e[4 + i] = exp2f(sc[2 * p + 1][i] * cs - mxs);
    }
    u32x4 u;
#pragma unroll
    for (int i = 0; i < 4; ++i) u[i] = pack2(e[2 * i], e[2 * i + 1]);
    pf[p] = *reinterpret_cast<bf16x8*>(&u);
#pragma unroll
    for (int i = 0; i < 8; ++i) sum += e[i];
  }
  sum += __shfl_xor(sum, 16, 64);
  sum += __shfl_xor(sum, 32, 64);
  const float inv = 1.f / sum;

  f32x4 oacc[2] = {f32x4{0.f, 0.f, 0.f, 0.f}, f32x4{0.f, 0.f, 0.f, 0.f}};
#pragma unroll
  for (int p = 0; p < 9; ++p) {
    int k0 = kbase + 32 * p + 4 * g;
    int k1 = k0 + 16;
    k0 = k0 < 0 ? 0 : k0;
    k1 = k1 < 0 ? 0 : (k1 > S_ - 4 ? S_ - 4 : k1);
#pragma unroll
    for (int dt = 0; dt < 2; ++dt) {
      const unsigned short* vr = vt + (size_t)(dt * 16 + l15) * S_;
      u32x2 v0 = *reinterpret_cast<const u32x2*>(vr + k0);
      u32x2 v1 = *reinterpret_cast<const u32x2*>(vr + k1);
      u32x4 u = u32x4{v0[0], v0[1], v1[0], v1[1]};
      oacc[dt] = __builtin_amdgcn_mfma_f32_16x16x32_bf16(*reinterpret_cast<bf16x8*>(&u), pf[p], oacc[dt], 0, 0, 0);
    }
  }
  unsigned short* ob = reinterpret_cast<unsigned short*>(ws + WS_O);
#pragma unroll
  for (int dt = 0; dt < 2; ++dt) {
    const int d = dt * 16 + 4 * g;
    if (d < DH) {
      u32x2 v; v[0] = pack2(oacc[dt][0] * inv, oacc[dt][1] * inv); v[1] = pack2(oacc[dt][2] * inv, oacc[dt][3] * inv);
      *reinterpret_cast<u32x2*>(ob + (size_t)(q0 + l15) * D_ + h * DH + d) = v;
    }
  }
}

constexpr int K3_WS = 64 + 8;
__global__ __launch_bounds__(256) void k3_out(const unsigned short* __restrict__ x,
                                               const unsigned short* __restrict__ wo,
                                               const unsigned char* __restrict__ ws,
                                               unsigned short* __restrict__ out) {
  __shared__ __attribute__((aligned(16))) unsigned short sW[D_ * K3_WS];
  const int tid = threadIdx.x, lane = tid & 63, wave = tid >> 6;
  const int g = lane >> 4, l15 = lane & 15;
  const int row0 = blockIdx.x * 64, col0 = blockIdx.y * 64;
  {
    u32x4 w[14];
#pragma unroll
    for (int i = 0; i < 14; ++i) {
      int c = tid + i * 256; int r = c >> 3, cc = c & 7;
      w[i] = *reinterpret_cast<const u32x4*>(wo + (size_t)r * D_ + col0 + cc * 8);
    }
#pragma unroll
    for (int i = 0; i < 14; ++i) {
      int c = tid + i * 256; int r = c >> 3, cc = c & 7;
      *reinterpret_cast<u32x4*>(&sW[r * K3_WS + cc * 8]) = w[i];
    }
  }
  const unsigned short* ob = reinterpret_cast<const unsigned short*>(ws + WS_O);
  const int myrow = row0 + wave * 16 + l15;
  bf16x8 a[14];
#pragma unroll
  for (int ks = 0; ks < 14; ++ks)
    a[ks] = *reinterpret_cast<const bf16x8*>(ob + (size_t)myrow * D_ + ks * 32 + g * 8);
  __syncthreads();
  f32x4 acc[4];
#pragma unroll
  for (int n = 0; n < 4; ++n) acc[n] = f32x4{0.f, 0.f, 0.f, 0.f};
#pragma unroll
  for (int ks = 0; ks < 14; ++ks) {
#pragma unroll
    for (int n = 0; n < 4; ++n) {
      const int q = l15 >> 2, p = l15 & 3;
      const unsigned short* base = &sW[(ks * 32 + 8 * g + q) * K3_WS + n * 16 + p * 4];
      s16x4 t0 = __builtin_amdgcn_ds_read_tr16_b64_v4i16((__attribute__((address_space(3))) s16x4*)(base));
      s16x4 t1 = __builtin_amdgcn_ds_read_tr16_b64_v4i16((__attribute__((address_space(3))) s16x4*)(base + 4 * K3_WS));
      bf16x8 b = bf16x8{t0[0], t0[1], t0[2], t0[3], t1[0], t1[1], t1[2], t1[3]};
      acc[n] = __builtin_amdgcn_mfma_f32_16x16x32_bf16(b, a[ks], acc[n], 0, 0, 0);
    }
  }
  const float2 ms = reinterpret_cast<const float2*>(ws + WS_ST)[myrow];
  const int* selp = reinterpret_cast<const int*>(ws + WS_SEL);
  const int sel0 = selp[0], sel1 = selp[1];
  const int tsel = (myrow == sel0) ? 0 : ((myrow == sel1) ? 1 : -1);
  const float* mp = reinterpret_cast<const float*>(ws + WS_MP);
#pragma unroll
  for (int n = 0; n < 4; ++n) {
    const int c = col0 + n * 16 + 4 * g;
    u32x2 xv = *reinterpret_cast<const u32x2*>(x + (size_t)myrow * D_ + c);
    float xf[4] = {bf2f(xv[0] & 0xffff), bf2f(xv[0] >> 16), bf2f(xv[1] & 0xffff), bf2f(xv[1] >> 16)};
    float r[4];
#pragma unroll
    for (int i = 0; i < 4; ++i) {
      float xn = bf2f(f2bf((xf[i] - ms.x) * ms.y));
      r[i] = xf[i] + acc[n][i] + xn;
    }
    if (tsel >= 0) {
      float y[4] = {0.f, 0.f, 0.f, 0.f};
      for (int sl = 0; sl < MOE_NS; ++sl) {
        float4 v = *reinterpret_cast<const float4*>(mp + ((size_t)sl * 2 + tsel) * D_ + c);
        y[0] += v.x; y[1] += v.y; y[2] += v.z; y[3] += v.w;
      }
#pragma unroll
      for (int i = 0; i < 4; ++i) r[i] += y[i];
    }
    u32x2 v; v[0] = pack2(r[0], r[1]); v[1] = pack2(r[2], r[3]);
    *reinterpret_cast<u32x2*>(out + (size_t)myrow * D_ + c) = v;
  }
}

extern "C" void kernel_launch(void* const* d_in, const int* in_sizes, int n_in,
                              void* d_out, int out_size, void* d_ws, size_t ws_size,
                              hipStream_t stream) {
  const unsigned short* x = (const unsigned short*)d_in[0];
  const unsigned short* wqkv = (const unsigned short*)d_in[1];
  const unsigned short* wo = (const unsigned short*)d_in[2];
  const unsigned short* w_r = (const unsigned short*)d_in[4];
  const unsigned short* w_gate = (const unsigned short*)d_in[5];
  const unsigned short* w_up = (const unsigned short*)d_in[6];
  const unsigned short* w_down = (const unsigned short*)d_in[7];
  unsigned char* ws = (unsigned char*)d_ws;
  unsigned short* out = (unsigned short*)d_out;

  k1_ln_qkv<<<dim3(S_ / K1_BM, N3 / K1_BN), 256, 0, stream>>>(x, wqkv, w_r, ws);
  k2_attn<<<dim3(K2_ATT_BLOCKS + MOE_NS), 256, 0, stream>>>(x, w_gate, w_up, w_down, ws);
  k3_out<<<dim3(S_ / 64, D_ / 64), 256, 0, stream>>>(x, wo, ws, out);
}
```

```cpp
#include <hip/hip_runtime.h>
#include <hip/hip_bf16.h>
#include <stdint.h>

constexpr int S_ = 2048, D_ = 448, H_ = 16, DH = 28, WIN = 256, F_ = 1792;
constexpr int N3 = 3 * D_;

typedef short bf16x8 __attribute__((ext_vector_type(8)));
typedef short s16x4 __attribute__((ext_vector_type(4)));
typedef float f32x4 __attribute__((ext_vector_type(4)));
typedef unsigned u32x4 __attribute__((ext_vector_type(4)));
typedef unsigned u32x2 __attribute__((ext_vector_type(2)));

__device__ __forceinline__ float bf2f(unsigned short u) { return __uint_as_float(((unsigned)u) << 16); }
__device__ __forceinline__ unsigned short f2bf(float f) {
  return __builtin_bit_cast(unsigned short, (__bf16)f);
}
__device__ __forceinline__ unsigned pack2(float lo, float hi) {
  return (unsigned)f2bf(lo) | ((unsigned)f2bf(hi) << 16);
}
__device__ __forceinline__ float wave_sum(float v) {
#pragma unroll
  for (int o = 32; o >= 1; o >>= 1) v += __shfl_xor(v, o, 64);
  return v;
}

constexpr size_t WS_QB = 0;
constexpr size_t WS_KB = WS_QB + (size_t)H_ * S_ * 32 * 2;
constexpr size_t WS_VT = WS_KB + (size_t)H_ * S_ * 32 * 2;
constexpr size_t WS_O  = WS_VT + (size_t)H_ * 32 * 2112 * 2;
constexpr size_t WS_ST = WS_O + (size_t)S_ * D_ * 2;
constexpr size_t WS_R  = WS_ST + (size_t)S_ * 8;
constexpr int MOE_NS = 56;
constexpr size_t WS_MP = WS_R + (size_t)S_ * 4;
constexpr size_t WS_SEL = WS_MP + (size_t)MOE_NS * 2 * D_ * 4;

constexpr int K1_BM = 64, K1_BN = 64;
constexpr int K1_WS = K1_BN + 8;

__global__ __launch_bounds__(256) void k1_ln_qkv(const unsigned short* __restrict__ x,
                                                  const unsigned short* __restrict__ wqkv,
                                                  const unsigned short* __restrict__ w_r,
                                                  unsigned char* __restrict__ ws) {
  __shared__ __attribute__((aligned(16))) unsigned short sW[D_ * K1_WS];
  const int tid = threadIdx.x, lane = tid & 63, wave = tid >> 6;
  const int g = lane >> 4, l15 = lane & 15;
  const int row0 = blockIdx.x * K1_BM;
  int col0 = blockIdx.y * 3 * K1_BN;
  const int myrow = row0 + wave * 16 + l15;

  u32x4 wreg[14];
#pragma unroll
  for (int i = 0; i < 14; ++i) {
    int c = tid + i * 256;
    int r = c >> 3, cc = c & 7;
    wreg[i] = *reinterpret_cast<const u32x4*>(wqkv + (size_t)r * N3 + col0 + cc * 8);
  }
  u32x4 xr[14];
#pragma unroll
  for (int ks = 0; ks < 14; ++ks)
    xr[ks] = *reinterpret_cast<const u32x4*>(x + (size_t)myrow * D_ + ks * 32 + g * 8);

  float s = 0.f;
#pragma unroll
  for (int ks = 0; ks < 14; ++ks)
#pragma unroll
    for (int j = 0; j < 4; ++j) s += bf2f(xr[ks][j] & 0xffff) + bf2f(xr[ks][j] >> 16);
  s += __shfl_xor(s, 16, 64); s += __shfl_xor(s, 32, 64);
  const float mean = s * (1.f / D_);
  float q = 0.f;
#pragma unroll
  for (int ks = 0; ks < 14; ++ks)
#pragma unroll
    for (int j = 0; j < 4; ++j) {
      float d0 = bf2f(xr[ks][j] & 0xffff) - mean, d1 = bf2f(xr[ks][j] >> 16) - mean;
      q += d0 * d0 + d1 * d1;
    }
  q += __shfl_xor(q, 16, 64); q += __shfl_xor(q, 32, 64);
  const float rstd = rsqrtf(q * (1.f / D_) + 1e-5f);
#pragma unroll
  for (int ks = 0; ks < 14; ++ks)
#pragma unroll
    for (int j = 0; j < 4; ++j)
      xr[ks][j] = pack2((bf2f(xr[ks][j] & 0xffff) - mean) * rstd, (bf2f(xr[ks][j] >> 16) - mean) * rstd);
  if (blockIdx.y == 0) {
    float rr = 0.f;
#pragma unroll
    for (int ks = 0; ks < 14; ++ks) {
      u32x4 w4 = *reinterpret_cast<const u32x4*>(w_r + ks * 32 + g * 8);
#pragma unroll
      for (int j = 0; j < 4; ++j)
        rr += bf2f(xr[ks][j] & 0xffff) * bf2f(w4[j] & 0xffff) + bf2f(xr[ks][j] >> 16) * bf2f(w4[j] >> 16);
    }
    rr += __shfl_xor(rr, 16, 64); rr += __shfl_xor(rr, 32, 64);
    if (g == 0) {
      reinterpret_cast<float*>(ws + WS_R)[myrow] = rr;
      reinterpret_cast<float2*>(ws + WS_ST)[myrow] = make_float2(mean, rstd);
    }
  }
#pragma unroll
  for (int t = 0; t < 3; ++t, col0 += K1_BN) {
  if (t) __syncthreads();
#pragma unroll
  for (int i = 0; i < 14; ++i) {
    int c = tid + i * 256;
    int r = c >> 3, cc = c & 7;
    *reinterpret_cast<u32x4*>(&sW[r * K1_WS + cc * 8]) = wreg[i];
  }
  __syncthreads();
  if (t < 2) {
#pragma unroll
    for (int i = 0; i < 14; ++i) {
      int c = tid + i * 256; int r = c >> 3, cc = c & 7;
      wreg[i] = *reinterpret_cast<const u32x4*>(wqkv + (size_t)r * N3 + col0 + K1_BN + cc * 8);
    }
  }

  f32x4 acc[4];
#pragma unroll
  for (int n = 0; n < 4; ++n) acc[n] = f32x4{0.f, 0.f, 0.f, 0.f};
  if (col0 < 2 * D_) {
#pragma unroll
    for (int ks = 0; ks < 14; ++ks) {
      const bf16x8 a = *reinterpret_cast<const bf16x8*>(&xr[ks]);
      bf16x8 b[4];
#pragma unroll
      for (int n = 0; n < 4; ++n) {
        const int q4 = l15 >> 2, p = l15 & 3;
        const unsigned short* base = &sW[(ks * 32 + 8 * g + q4) * K1_WS + n * 16 + p * 4];
        s16x4 t0 = __builtin_amdgcn_ds_read_tr16_b64_v4i16((__attribute__((address_space(3))) s16x4*)(base));
        s16x4 t1 = __builtin_amdgcn_ds_read_tr16_b64_v4i16((__attribute__((address_space(3))) s16x4*)(base + 4 * K1_WS));
        b[n] = bf16x8{t0[0], t0[1], t0[2], t0[3], t1[0], t1[1], t1[2], t1[3]};
      }
#pragma unroll
      for (int n = 0; n < 4; ++n) acc[n] = __builtin_amdgcn_mfma_f32_16x16x32_bf16(b[n], a, acc[n], 0, 0, 0);
    }
  } else {
#pragma unroll
    for (int ks = 0; ks < 14; ++ks) {
      const bf16x8 a = *reinterpret_cast<const bf16x8*>(&xr[ks]);
      bf16x8 b[4];
#pragma unroll
      for (int n = 0; n < 4; ++n) {
        const int q4 = l15 >> 2, p = l15 & 3;
        const unsigned short* base = &sW[(ks * 32 + 8 * g + q4) * K1_WS + n * 16 + p * 4];
        s16x4 t0 = __builtin_amdgcn_ds_read_tr16_b64_v4i16((__attribute__((address_space(3))) s16x4*)(base));
        s16x4 t1 = __builtin_amdgcn_ds_read_tr16_b64_v4i16((__attribute__((address_space(3))) s16x4*)(base + 4 * K1_WS));
        b[n] = bf16x8{t0[0], t0[1], t0[2], t0[3], t1[0], t1[1], t1[2], t1[3]};
      }
#pragma unroll
      for (int n = 0; n < 4; ++n) acc[n] = __builtin_amdgcn_mfma_f32_16x16x32_bf16(a, b[n], acc[n], 0, 0, 0);
    }
  }

  unsigned short* qb = reinterpret_cast<unsigned short*>(ws + WS_QB);
  unsigned short* vt = reinterpret_cast<unsigned short*>(ws + WS_VT);
#pragma unroll
  for (int n = 0; n < 4; ++n) {
    const int ncol = col0 + n * 16;
    u32x2 v; v[0] = pack2(acc[n][0], acc[n][1]); v[1] = pack2(acc[n][2], acc[n][3]);
    if (ncol < 2 * D_) {
      const int nn = ncol + 4 * g;
      const int part = nn / D_;
      const int rem = nn - part * D_;
      const int h = rem / DH, d = rem - h * DH;
      unsigned short* dst = qb + (size_t)part * (H_ * S_ * 32) + (size_t)h * S_ * 32;
      *reinterpret_cast<u32x2*>(dst + (size_t)myrow * 32 + d) = v;
      if (d == 24) *reinterpret_cast<u32x2*>(dst + (size_t)myrow * 32 + 28) = u32x2{0u, 0u};
    } else {
      const int rem = ncol + l15 - 2 * D_;
      const int h = rem / DH, d = rem - h * DH;
      unsigned short* dst = vt + ((size_t)h * 32 + d) * 2112;
      *reinterpret_cast<u32x2*>(dst + row0 + wave * 16 + 4 * g) = v;
    }
  }
  }
}

constexpr int K2_ATT_BLOCKS = (S_ / 64) * H_;

__device__ void moe_block(int slice, const unsigned short* __restrict__ x,
                          const unsigned short* __restrict__ w_gate, const unsigned short* __restrict__ w_up,
                          const unsigned short* __restrict__ w_down, unsigned char* __restrict__ ws) {
  __shared__ float s_red[4][4];
  __shared__ int s_redi[4][2];
  __shared__ float s_xn[2][D_];
  __shared__ float s_part[16][4][32];
  __shared__ float s_h[2][32];
  const int tid = threadIdx.x, lane = tid & 63, wave = tid >> 6;
  const int f0 = slice * 32;
  const float* r = reinterpret_cast<const float*>(ws + WS_R);
  const int kg_ = tid >> 4, cp_ = tid & 15;
  unsigned wg_[28], wu_[28], wd_[32];
#pragma unroll
  for (int kk = 0; kk < 28; ++kk) {
    int k = kg_ * 28 + kk;
    wg_[kk] = *reinterpret_cast<const unsigned*>(w_gate + (size_t)k * F_ + f0 + cp_ * 2);
    wu_[kk] = *reinterpret_cast<const unsigned*>(w_up + (size_t)k * F_ + f0 + cp_ * 2);
  }
#pragma unroll
  for (int f = 0; f < 32; ++f)
    wd_[f] = (tid < D_ / 2) ? *reinterpret_cast<const unsigned*>(w_down + (size_t)(f0 + f) * D_ + tid * 2) : 0u;
  float b1 = -INFINITY, b2 = -INFINITY; int i1 = 0x7fffffff, i2 = 0x7fffffff;
#pragma unroll
  for (int i = 0; i < 8; ++i) {
    int idx = tid * 8 + i;
    float v = r[idx];
    if (v > b1) { b2 = b1; i2 = i1; b1 = v; i1 = idx; }
    else if (v > b2) { b2 = v; i2 = idx; }
  }
#pragma unroll
  for (int o = 1; o < 64; o <<= 1) {
    float c1 = __shfl_xor(b1, o, 64), c2 = __shfl_xor(b2, o, 64);
    int j1 = __shfl_xor(i1, o, 64), j2 = __shfl_xor(i2, o, 64);
    bool cfirst = (c1 > b1) || (c1 == b1 && j1 < i1);
    float n1 = cfirst ? c1 : b1; int m1 = cfirst ? j1 : i1;
    float xa = cfirst ? b1 : c1; int ia = cfirst ? i1 : j1;
    float xb = cfirst ? c2 : b2; int ib = cfirst ? j2 : i2;
    bool asec = (xa > xb) || (xa == xb && ia < ib);
    b1 = n1; i1 = m1; b2 = asec ? xa : xb; i2 = asec ? ia : ib;
  }
  if (lane == 0) { s_red[wave][0] = b1; s_red[wave][1] = b2; s_redi[wave][0] = i1; s_redi[wave][1] = i2; }
  __syncthreads();
  b1 = s_red[0][0]; b2 = s_red[0][1]; i1 = s_redi[0][0]; i2 = s_redi[0][1];
#pragma unroll
  for (int w = 1; w < 4; ++w) {
    float c1 = s_red[w][0], c2 = s_red[w][1]; int j1 = s_redi[w][0], j2 = s_redi[w][1];
    bool cfirst = (c1 > b1) || (c1 == b1 && j1 < i1);
    float n1 = cfirst ? c1 : b1; int m1 = cfirst ? j1 : i1;
    float xa = cfirst ? b1 : c1; int ia = cfirst ? i1 : j1;
    float xb = cfirst ? c2 : b2; int ib = cfirst ? j2 : i2;
    bool asec = (xa > xb) || (xa == xb && ia < ib);
    b1 = n1; i1 = m1; b2 = asec ? xa : xb; i2 = asec ? ia : ib;
  }
  const int sel[2] = {i1, i2};
  const float rs[2] = {b1, b2};
  const float2* st = reinterpret_cast<const float2*>(ws + WS_ST);
  for (int e = tid; e < 2 * D_; e += 256) {
    int t = e / D_, c = e - t * D_;
    float2 ms = st[sel[t]];
    float v = (bf2f(x[(size_t)sel[t] * D_ + c]) - ms.x) * ms.y;
    s_xn[t][c] = bf2f(f2bf(v));
  }
  __syncthreads();
  {
    const int kg = tid >> 4, cp = tid & 15;
    float a[4][2] = {{0, 0}, {0, 0}, {0, 0}, {0, 0}};
#pragma unroll
    for (int kk = 0; kk < 28; ++kk) {
      int k = kg * 28 + kk;
      unsigned wg = wg_[kk];
      unsigned wu = wu_[kk];
      float g0 = bf2f(wg & 0xffff), g1 = bf2f(wg >> 16), u0 = bf2f(wu & 0xffff), u1 = bf2f(wu >> 16);
      float x0 = s_xn[0][k], x1 = s_xn[1][k];
      a[0][0] += x0 * g0; a[0][1] += x0 * g1; a[1][0] += x0 * u0; a[1][1] += x0 * u1;
      a[2][0] += x1 * g0; a[2][1] += x1 * g1; a[3][0] += x1 * u0; a[3][1] += x1 * u1;
    }
#pragma unroll
    for (int j = 0; j < 4; ++j) { s_part[kg][j][cp * 2] = a[j][0]; s_part[kg][j][cp * 2 + 1] = a[j][1]; }
  }
  __syncthreads();
  if (tid < 64) {
    int t = tid >> 5, c = tid & 31;
    float gs = 0.f, us = 0.f;
#pragma unroll
    for (int kg = 0; kg < 16; ++kg) { gs += s_part[kg][t * 2][c]; us += s_part[kg][t * 2 + 1][c]; }
    float sg = gs / (1.f + __expf(-gs));
    s_h[t][c] = sg * us;
  }
  __syncthreads();
  if (tid < D_ / 2) {
    float y[2][2] = {{0, 0}, {0, 0}};
#pragma unroll
    for (int f = 0; f < 32; ++f) {
      unsigned wd = wd_[f];
      float d0 = bf2f(wd & 0xffff), d1 = bf2f(wd >> 16);
      y[0][0] += s_h[0][f] * d0; y[0][1] += s_h[0][f] * d1;
      y[1][0] += s_h[1][f] * d0; y[1][1] += s_h[1][f] * d1;
    }
    float* mp = reinterpret_cast<float*>(ws + WS_MP) + (size_t)slice * 2 * D_;
#pragma unroll
    for (int t = 0; t < 2; ++t) {
      float sc = 1.f / (1.f + __expf(-rs[t]));
      *reinterpret_cast<float2*>(mp + t * D_ + tid * 2) = make_float2(y[t][0] * sc, y[t][1] * sc);
    }
  }
  if (slice == 0 && tid == 0) {
    int* selp = reinterpret_cast<int*>(ws + WS_SEL);
    selp[0] = sel[0]; selp[1] = sel[1];
  }
}

__global__ __launch_bounds__(256) void k2_attn(const unsigned short* __restrict__ x,
                                                const unsigned short* __restrict__ w_gate,
                                                const unsigned short* __restrict__ w_up,
                                                const unsigned short* __restrict__ w_down,
                                                unsigned char* __restrict__ ws) {
  if (blockIdx.x < MOE_NS) {
    moe_block(blockIdx.x, x, w_gate, w_up, w_down, ws);
    return;
  }
  const int tid = threadIdx.x, lane = tid & 63, wave = tid >> 6;
  const int g = lane >> 4, l15 = lane & 15;
  const int h = (blockIdx.x - MOE_NS) & 15;
  const int qt = (blockIdx.x - MOE_NS) >> 4;
  const int q0 = qt * 64 + wave * 16;
  const unsigned short* qb = reinterpret_cast<const unsigned short*>(ws + WS_QB) + (size_t)h * S_ * 32;
  const unsigned short* kb = reinterpret_cast<const unsigned short*>(ws + WS_KB) + (size_t)h * S_ * 32;
  const unsigned short* vt = reinterpret_cast<const unsigned short*>(ws + WS_VT) + (size_t)h * 32 * 2112;

  const bf16x8 qf = *reinterpret_cast<const bf16x8*>(qb + (size_t)(q0 + l15) * 32 + g * 8);
  const int kbase = q0 - WIN;
  bf16x8 kf[17];
#pragma unroll
  for (int j = 0; j < 17; ++j) {
    int key = kbase + 16 * j + l15;
    key = key < 0 ? 0 : key;
    kf[j] = *reinterpret_cast<const bf16x8*>(kb + (size_t)key * 32 + g * 8);
  }
  u32x2 vv[9][2][2];
#pragma unroll
  for (int p = 0; p < 9; ++p) {
    int k0 = kbase + 32 * p + 4 * g;
    int k1 = k0 + 16;
    k0 = k0 < 0 ? 0 : k0;
    k1 = k1 < 0 ? 0 : (k1 > S_ - 4 ? S_ - 4 : k1);
#pragma unroll
    for (int dt = 0; dt < 2; ++dt) {
      const unsigned short* vr = vt + (size_t)(dt * 16 + l15) * 2112;
      vv[p][dt][0] = *reinterpret_cast<const u32x2*>(vr + k0);
      vv[p][dt][1] = *reinterpret_cast<const u32x2*>(vr + k1);
    }
  }
  f32x4 sc[18];
#pragma unroll
  for (int j = 0; j < 17; ++j)
    sc[j] = __builtin_amdgcn_mfma_f32_16x16x32_bf16(kf[j], qf, f32x4{0.f, 0.f, 0.f, 0.f}, 0, 0, 0);
  float mx = -INFINITY;
#pragma unroll
  for (int j = 0; j < 17; ++j) {
    const bool tile_ok = (kbase + 16 * j) >= 0;
#pragma unroll
    for (int i = 0; i < 4; ++i) {
      const int b = 4 * g + i;
      bool ok = tile_ok;
      if (j == 0) ok = ok && (b >= l15 + 1);
      if (j == 16) ok = ok && (b <= l15);
      sc[j][i] = ok ? sc[j][i] : -INFINITY;
      mx = fmaxf(mx, sc[j][i]);
    }
  }
  sc[17] = f32x4{-INFINITY, -INFINITY, -INFINITY, -INFINITY};
  mx = fmaxf(mx, __shfl_xor(mx, 16, 64));
  mx = fmaxf(mx, __shfl_xor(mx, 32, 64));
  const float cs = 0.18898223650461362f * 1.4426950408889634f;
  const float mxs = mx * cs;
  float sum = 0.f;
  bf16x8 pf[9];
#pragma unroll
  for (int p = 0; p < 9; ++p) {
    float e[8];
#pragma unroll
    for (int i = 0; i < 4; ++i) {
      e[i] = exp2f(sc[2 * p][i] * cs - mxs);
      e[4 + i] = exp2f(sc[2 * p + 1][i] * cs - mxs);
    }
    u32x4 u;
#pragma unroll
    for (int i = 0; i < 4; ++i) u[i] = pack2(e[2 * i], e[2 * i + 1]);
    pf[p] = *reinterpret_cast<bf16x8*>(&u);
#pragma unroll
    for (int i = 0; i < 8; ++i) sum += e[i];
  }
  sum += __shfl_xor(sum, 16, 64);
  sum += __shfl_xor(sum, 32, 64);
  const float inv = 1.f / sum;

  f32x4 oacc[2] = {f32x4{0.f, 0.f, 0.f, 0.f}, f32x4{0.f, 0.f, 0.f, 0.f}};
#pragma unroll
  for (int p = 0; p < 9; ++p) {
#pragma unroll
    for (int dt = 0; dt < 2; ++dt) {
      u32x2 v0 = vv[p][dt][0];
      u32x2 v1 = vv[p][dt][1];
      u32x4 u = u32x4{v0[0], v0[1], v1[0], v1[1]};
      oacc[dt] = __builtin_amdgcn_mfma_f32_16x16x32_bf16(*reinterpret_cast<bf16x8*>(&u), pf[p], oacc[dt], 0, 0, 0);
    }
  }
  unsigned short* ob = reinterpret_cast<unsigned short*>(ws + WS_O);
#pragma unroll
  for (int dt = 0; dt < 2; ++dt) {
    const int d = dt * 16 + 4 * g;
    if (d < DH) {
      u32x2 v; v[0] = pack2(oacc[dt][0] * inv, oacc[dt][1] * inv); v[1] = pack2(oacc[dt][2] * inv, oacc[dt][3] * inv);
      *reinterpret_cast<u32x2*>(ob + (size_t)(q0 + l15) * D_ + h * DH + d) = v;
    }
  }
}

constexpr int K3_WS = 64 + 8;
__global__ __launch_bounds__(256) void k3_out(const unsigned short* __restrict__ x,
                                               const unsigned short* __restrict__ wo,
                                               const unsigned char* __restrict__ ws,
                                               unsigned short* __restrict__ out) {
  __shared__ __attribute__((aligned(16))) unsigned short sW[D_ * K3_WS];
  __shared__ float s_moe[4][2][64];
  const int tid = threadIdx.x, lane = tid & 63, wave = tid >> 6;
  const int g = lane >> 4, l15 = lane & 15;
  const int row0 = blockIdx.x * 64, col0 = blockIdx.y * 64;
  const int* selp = reinterpret_cast<const int*>(ws + WS_SEL);
  const int sel0 = selp[0], sel1 = selp[1];
  const float* mp = reinterpret_cast<const float*>(ws + WS_MP);
  const bool has0 = (sel0 >= row0) && (sel0 < row0 + 64), has1 = (sel1 >= row0) && (sel1 < row0 + 64);
  if (has0 || has1) {
    float y0 = 0.f, y1 = 0.f;
#pragma unroll
    for (int i = 0; i < 14; ++i) {
      int sl = wave * 14 + i;
      y0 += mp[((size_t)sl * 2 + 0) * D_ + col0 + lane];
      y1 += mp[((size_t)sl * 2 + 1) * D_ + col0 + lane];
    }
    s_moe[wave][0][lane] = y0; s_moe[wave][1][lane] = y1;
  }
  {
    u32x4 w[14];
#pragma unroll
    for (int i = 0; i < 14; ++i) {
      int c = tid + i * 256; int r = c >> 3, cc = c & 7;
      w[i] = *reinterpret_cast<const u32x4*>(wo + (size_t)r * D_ + col0 + cc * 8);
    }
#pragma unroll
    for (int i = 0; i < 14; ++i) {
      int c = tid + i * 256; int r = c >> 3, cc = c & 7;
      *reinterpret_cast<u32x4*>(&sW[r * K3_WS + cc * 8]) = w[i];
    }
  }
  const unsigned short* ob = reinterpret_cast<const unsigned short*>(ws + WS_O);
  const int myrow = row0 + wave * 16 + l15;
  bf16x8 a[14];
#pragma unroll
  for (int ks = 0; ks < 14; ++ks)
    a[ks] = *reinterpret_cast<const bf16x8*>(ob + (size_t)myrow * D_ + ks * 32 + g * 8);
  __syncthreads();
  f32x4 acc[4];
#pragma unroll
  for (int n = 0; n < 4; ++n) acc[n] = f32x4{0.f, 0.f, 0.f, 0.f};
#pragma unroll
  for (int ks = 0; ks < 14; ++ks) {
#pragma unroll
    for (int n = 0; n < 4; ++n) {
      const int q = l15 >> 2, p = l15 & 3;
      const unsigned short* base = &sW[(ks * 32 + 8 * g + q) * K3_WS + n * 16 + p * 4];
      s16x4 t0 = __builtin_amdgcn_ds_read_tr16_b64_v4i16((__attribute__((address_space(3))) s16x4*)(base));
      s16x4 t1 = __builtin_amdgcn_ds_read_tr16_b64_v4i16((__attribute__((address_space(3))) s16x4*)(base + 4 * K3_WS));
      bf16x8 b = bf16x8{t0[0], t0[1], t0[2], t0[3], t1[0], t1[1], t1[2], t1[3]};
      acc[n] = __builtin_amdgcn_mfma_f32_16x16x32_bf16(b, a[ks], acc[n], 0, 0, 0);
    }
  }
  const float2 ms = reinterpret_cast<const float2*>(ws + WS_ST)[myrow];
  const int tsel = (myrow == sel0) ? 0 : ((myrow == sel1) ? 1 : -1);
#pragma unroll
  for (int n = 0; n < 4; ++n) {
    const int c = col0 + n * 16 + 4 * g;
    u32x2 xv = *reinterpret_cast<const u32x2*>(x + (size_t)myrow * D_ + c);
    float xf[4] = {bf2f(xv[0] & 0xffff), bf2f(xv[0] >> 16), bf2f(xv[1] & 0xffff), bf2f(xv[1] >> 16)};
    float r[4];
#pragma unroll
    for (int i = 0; i < 4; ++i) {
      float xn = bf2f(f2bf((xf[i] - ms.x) * ms.y));
      r[i] = xf[i] + acc[n][i] + xn;
    }
    if (tsel >= 0) {
#pragma unroll
      for (int i = 0; i < 4; ++i) {
        const int cl = n * 16 + 4 * g + i;
        r[i] += (s_moe[0][tsel][cl] + s_moe[1][tsel][cl]) + (s_moe[2][tsel][cl] + s_moe[3][tsel][cl]);
      }
    }
    u32x2 v; v[0] = pack2(r[0], r[1]); v[1] = pack2(r[2], r[3]);
    *reinterpret_cast<u32x2*>(out + (size_t)myrow * D_ + c) = v;
  }
}

extern "C" void kernel_launch(void* const* d_in, const int* in_sizes, int n_in,
                              void* d_out, int out_size, void* d_ws, size_t ws_size,
                              hipStream_t stream) {
  const unsigned short* x = (const unsigned short*)d_in[0];
  const unsigned short* wqkv = (const unsigned short*)d_in[1];
  const unsigned short* wo = (const unsigned short*)d_in[2];
  const unsigned short* w_r = (const unsigned short*)d_in[4];
  const unsigned short* w_gate = (const unsigned short*)d_in[5];
  const unsigned short* w_up = (const unsigned short*)d_in[6];
  const unsigned short* w_down = (const unsigned short*)d_in[7];
  unsigned char* ws = (unsigned char*)d_ws;
  unsigned short* out = (unsigned short*)d_out;

  k1_ln_qkv<<<dim3(S_ / K1_BM, N3 / K1_BN / 3), 256, 0, stream>>>(x, wqkv, w_r, ws);
  k2_attn<<<dim3(K2_ATT_BLOCKS + MOE_NS), 256, 0, stream>>>(x, w_gate, w_up, w_down, ws);
  k3_out<<<dim3(S_ / 64, D_ / 64), 256, 0, stream>>>(x, wo, ws, out);
}
```
